```python
import torch

def _softmax(x, axis=-1):
    return torch.softmax(x, dim=axis)
def _gelu(x):
    return torch.nn.functional.gelu(x, approximate="tanh")
def _silu(x):
    return torch.nn.functional.silu(x)
def _sigmoid(x):
    return torch.sigmoid(x)
def _layernorm(x, axis=-1, eps=1e-5):
    m = x.mean(dim=axis, keepdim=True)
    v = x.var(dim=axis, keepdim=True, unbiased=False)
    return (x - m) / torch.sqrt(v + eps)
def _scatter_add(tgt, idx, src, axis=0):
    return tgt.clone().index_add_(axis, idx, src)
def _segment_sum(src, seg, n):
    out = torch.zeros((n,) + src.shape[1:], dtype=src.dtype)
    return out.index_add_(0, seg, src)
def _pad(x, pw):
    flat = tuple(v for pair in reversed(pw) for v in pair)
    return torch.nn.functional.pad(x, flat)

kernel_name = 'recurrent_scan__gqa_block_cached__dense_activation_relu__decode'

def setup_inputs():

    _w = (lambda a: (lambda t: t.to(torch.bfloat16) if t.is_floating_point() else t)(torch.as_tensor(a)))
    def _A_setup():
        B, Sq, D = 1, 1, 512
        Hq, Hkv, Sk, Dh = 32, 8, 65536, 128
        assert Hq % Hkv == 0, (Hq, Hkv)
        return {
            "x": _w((lambda *s: torch.randn(*s, dtype=torch.bfloat16) * 0.05)(B, Sq, D)),
            "k_cache": _w((lambda *s: torch.randn(*s, dtype=torch.bfloat16) * 0.8)(B, Hkv, Sk, Dh)),
            "v_cache": _w((lambda *s: torch.randn(*s, dtype=torch.bfloat16) * 0.8)(B, Hkv, Sk, Dh)),
            "wq": _w((lambda *s: torch.randn(*s, dtype=torch.bfloat16) * 0.8)(D, Hq * Dh) * (D ** -0.5)),
            "wo": _w((lambda *s: torch.randn(*s, dtype=torch.bfloat16) * 0.8)(Hq * Dh, D) * ((Hq * Dh) ** -0.5)),
        }
    _A_in = _A_setup()

    def _B_setup():
        B, S, D, Dff = 1, 1, 512, 1792
        return {
            "x": _w((lambda *s: torch.randn(*s, dtype=torch.bfloat16) * 0.05)(B, S, D)),
            "w1": _w((lambda *s: torch.randn(*s, dtype=torch.bfloat16) * 0.2)(D, Dff)),
            "w2": _w((lambda *s: torch.randn(*s, dtype=torch.bfloat16) * 0.2)(Dff, D)),
        }
    _B_in = _B_setup()

    return {
        "A_x": _A_in["x"],
        "A_k_cache": _A_in["k_cache"],
        "A_v_cache": _A_in["v_cache"],
        "A_wq": _A_in["wq"],
        "A_wo": _A_in["wo"],
        "B_x": _B_in["x"],
        "B_w1": _B_in["w1"],
        "B_w2": _B_in["w2"],
    }

def reference(A_x, A_k_cache, A_v_cache, A_wq, A_wo, B_x, B_w1, B_w2):
    def _A_ref(x, k_cache, v_cache, wq, wo):
        B, Sq, D = x.shape
        Hq, Dh = 32, 128
        Hkv = k_cache.shape[1]
        rep = Hq // Hkv
        q = (lambda x, *d: x.permute(*d))((x @ wq).reshape(B, Sq, Hq, Dh), 0, 2, 1, 3)
        k = (lambda x, n, axis: torch.repeat_interleave(x, n, dim=axis))(k_cache, rep, axis=1)
        v = (lambda x, n, axis: torch.repeat_interleave(x, n, dim=axis))(v_cache, rep, axis=1)
        scores = torch.einsum("bhqd,bhkd->bhqk", q, k) * (Dh ** -0.5)
        p = _softmax(scores, axis=-1)
        o = torch.einsum("bhqk,bhkd->bhqd", p, v)
        return (lambda x, *d: x.permute(*d))(o, 0, 2, 1, 3).reshape(B, Sq, Hq * Dh) @ wo

    def _B_ref(x, w1, w2):
        h = torch.einsum("bsd,df->bsf", x, w1)
        zero = torch.zeros_like(h)
        h = torch.maximum(h, zero)
        return torch.einsum("bsf,fd->bsd", h, w2)

    x = A_x
    xn = _layernorm(x, axis=-1)
    s = _A_ref(xn, A_k_cache, A_v_cache, A_wq, A_wo)
    g = _silu(_B_ref(xn, B_w1, B_w2))
    return x + s * g
```

```cpp
#include <hip/hip_runtime.h>
#include <hip/hip_bf16.h>
#include <cstdio>
#include <cstdint>
#define GAS __attribute__((address_space(1)))
#define LAS __attribute__((address_space(3)))
typedef unsigned short bfr;
typedef unsigned v4u __attribute__((ext_vector_type(4)));
typedef float v4f __attribute__((ext_vector_type(4)));
typedef short v4s __attribute__((ext_vector_type(4)));
typedef __bf16 v8bf __attribute__((ext_vector_type(8)));
typedef __bf16 v2bf __attribute__((ext_vector_type(2)));

constexpr int D = 512, HQ = 32, HKV = 8, SK = 65536, DH = 128, DFF = 1792;
constexpr int NSPLIT = 32, NWAVE = 4, UNITS = SK / NSPLIT / 16 / NWAVE;
constexpr int NPIECE = 2 * UNITS;
#define RING_S 9
#define WIN 2
#define AUXK 2
#define AUXV 0
#define AMODE 2
constexpr int S = RING_S, W = WIN, A = S - 1;
constexpr float C2 = 0.08838834764831845f * 1.4426950408889634f;

__device__ __forceinline__ float b2f(bfr b) { return __builtin_bit_cast(float, (unsigned)b << 16); }
__device__ __forceinline__ bfr f2b(float f) { return __builtin_bit_cast(bfr, (__bf16)f); }
__device__ __forceinline__ float rb(float f) { return (float)(__bf16)f; }
__device__ __forceinline__ unsigned pk(float lo, float hi) { v2bf r = {(__bf16)lo, (__bf16)hi}; return __builtin_bit_cast(unsigned, r); }
__device__ __forceinline__ float xrow16_max(float x) {
  auto s = __builtin_amdgcn_permlane16_swap(__float_as_uint(x), __float_as_uint(x), false, false);
  x = fmaxf(__uint_as_float(s[0]), __uint_as_float(s[1]));
  auto t = __builtin_amdgcn_permlane32_swap(__float_as_uint(x), __float_as_uint(x), false, false);
  return fmaxf(__uint_as_float(t[0]), __uint_as_float(t[1]));
}
__device__ __forceinline__ float xrow16_sum(float x) {
  auto s = __builtin_amdgcn_permlane16_swap(__float_as_uint(x), __float_as_uint(x), false, false);
  x = __uint_as_float(s[0]) + __uint_as_float(s[1]);
  auto t = __builtin_amdgcn_permlane32_swap(__float_as_uint(x), __float_as_uint(x), false, false);
  return __uint_as_float(t[0]) + __uint_as_float(t[1]);
}
__device__ __forceinline__ float wave_sum(float v) {
#pragma unroll
    for (int m = 32; m >= 1; m >>= 1) v += __shfl_xor(v, m);
    return v;
}

constexpr size_t WS_Q = 0;
constexpr size_t WS_H = 8192;
constexpr size_t WS_PART = 16384;
constexpr size_t WS_SP = WS_PART + (size_t)HKV * NSPLIT * 4 * 132 * 4;

__device__ __forceinline__ void layernorm_block(const bfr* x, float* xn_lds, int tid) {
    const int lane = tid & 63;
    v4u xv = *(const v4u*)(x + lane * 8); float f[8];
#pragma unroll
    for (int i = 0; i < 4; ++i) { f[2 * i] = __builtin_bit_cast(float, xv[i] << 16); f[2 * i + 1] = __builtin_bit_cast(float, xv[i] & 0xffff0000u); }
    float s = 0; for (int i = 0; i < 8; ++i) s += f[i];
    const float mean = wave_sum(s) * (1.f / D);
    float v = 0; for (int i = 0; i < 8; ++i) { float d = f[i] - mean; v += d * d; }
    const float var = wave_sum(v) * (1.f / D);
    const float mb = rb(mean), sq = rb(sqrtf(rb(rb(var) + 1e-5f)));
    if (tid < 64) for (int i = 0; i < 8; ++i) xn_lds[lane * 8 + i] = rb(rb(f[i] - mb) / sq);
}

__global__ void __launch_bounds__(256) k_pre(const bfr* x, const bfr* wq, const bfr* w1, char* ws) {
    __shared__ float xn[D]; __shared__ float red[8][32];
    const int tid = threadIdx.x, b = blockIdx.x;
    layernorm_block(x, xn, tid); __syncthreads();
    { float acc[8] = {}; const int half = tid & 1, rg = tid >> 1;
      for (int i = 0; i < 4; ++i) { const int r = rg + 128 * i; v4u w = *(const v4u*)(wq + (size_t)r * 4096 + b * 16 + half * 8); const float xv = xn[r];
        for (int k = 0; k < 4; ++k) { acc[2 * k] += xv * __builtin_bit_cast(float, w[k] << 16); acc[2 * k + 1] += xv * __builtin_bit_cast(float, w[k] & 0xffff0000u); } }
      for (int k = 0; k < 8; ++k) { float v = acc[k]; for (int m = 2; m <= 32; m <<= 1) v += __shfl_xor(v, m); acc[k] = v; }
      if ((tid & 63) < 2) for (int k = 0; k < 8; ++k) red[tid >> 6][half * 8 + k] = acc[k];
      __syncthreads();
      if (tid < 16) ((bfr*)(ws + WS_Q))[b * 16 + tid] = f2b(red[0][tid] + red[1][tid] + red[2][tid] + red[3][tid]);
      __syncthreads(); }
    if (b < 224) { float acc[8] = {};
      for (int i = 0; i < 2; ++i) { const int r = tid + 256 * i; v4u w = *(const v4u*)(w1 + (size_t)r * DFF + b * 8); const float xv = xn[r];
        for (int k = 0; k < 4; ++k) { acc[2 * k] += xv * __builtin_bit_cast(float, w[k] << 16); acc[2 * k + 1] += xv * __builtin_bit_cast(float, w[k] & 0xffff0000u); } }
      for (int k = 0; k < 8; ++k) acc[k] = wave_sum(acc[k]);
      if ((tid & 63) == 0) for (int k = 0; k < 8; ++k) red[tid >> 6][k] = acc[k];
      __syncthreads();
      if (tid < 8) ((bfr*)(ws + WS_H))[b * 8 + tid] = f2b(fmaxf(rb(red[0][tid] + red[1][tid] + red[2][tid] + red[3][tid]), 0.f)); }
}

template <int N> __device__ __forceinline__ void wait_vm() { asm volatile("s_waitcnt vmcnt(%0) lgkmcnt(0)" :: "i"(N) : "memory"); }

__global__ void __launch_bounds__(256) k_attn(const char* __restrict__ K, const char* __restrict__ V, char* ws) {
    extern __shared__ __attribute__((aligned(16))) char lds[];
    const int tid = threadIdx.x, wave = __builtin_amdgcn_readfirstlane(tid >> 6), lane = tid & 63;
    const int h = blockIdx.x & 7, j = blockIdx.x >> 3;
    const int g = lane >> 4, c16 = lane & 15;
    const size_t u0 = AMODE == 2 ? (size_t)(j * NWAVE + wave) : (size_t)(j * (UNITS * NWAVE) + wave);
    const size_t ustride = AMODE == 2 ? (size_t)NSPLIT * NWAVE : NWAVE;
    unsigned koff[4], voff[4];
#pragma unroll
    for (int i = 0; i < 4; ++i) { const int row = 4 * i + g; koff[i] = row * 256 + ((c16 ^ row) << 4); voff[i] = row * 256 + ((c16 ^ ((row & 7) << 1)) << 4); }
    const char* kb = K + ((size_t)h * SK * 256 + u0 * 4096); const char* vb = V + ((size_t)h * SK * 256 + u0 * 4096);
    char* ring = lds + wave * (S * 4096);
    const unsigned ring_u = (unsigned)(size_t)(LAS char*)ring;
    auto issue = [&](int f, int slot) {
        const size_t go = (size_t)(f >> 1) * ustride * 4096;
        LAS char* dst = (LAS char*)ring + slot * 4096;
        if (f & 1) {
#pragma unroll
            for (int i = 0; i < 4; ++i) __builtin_amdgcn_global_load_lds((const GAS unsigned*)(vb + go + voff[i]), (LAS unsigned*)(dst + i * 1024), 16, 0, AUXV);
        } else {
#pragma unroll
            for (int i = 0; i < 4; ++i) __builtin_amdgcn_global_load_lds((const GAS unsigned*)(kb + go + koff[i]), (LAS unsigned*)(dst + i * 1024), 16, 0, AUXK);
        } };
    for (int f = 0; f < A; ++f) { if (f >= W) wait_vm<4 * (W - 1)>(); issue(f, f); }
    v8bf qf[4];
    { const bfr* q = (const bfr*)(ws + WS_Q) + (h * 4) * DH;
#pragma unroll
      for (int s = 0; s < 4; ++s) { v4u t = {0, 0, 0, 0}; if (c16 < 4) t = *(const v4u*)(q + c16 * DH + 32 * s + 8 * g); qf[s] = __builtin_bit_cast(v8bf, t); } }
    asm volatile("s_waitcnt vmcnt(0)" ::: "memory");
    unsigned kra[4];
#pragma unroll
    for (int s = 0; s < 4; ++s) kra[s] = c16 * 256 + (((4 * s + g) ^ c16) << 4);
    const int vrow = 4 * g + (c16 >> 2);
    const unsigned vra = vrow * 256 + ((vrow & 7) << 5) + 8 * (c16 & 3);
    float m = -INFINITY, l = 0.f; v4f o[8];
#pragma unroll
    for (int c = 0; c < 8; ++c) o[c] = v4f{0, 0, 0, 0};
    int cs = 0, is = A % S;
    for (int t = 0; t < UNITS; ++t) {
        const int f = 2 * t;
        if (f + A < NPIECE) { wait_vm<4 * (W - 1)>(); issue(f + A, is); is = is + 1 == S ? 0 : is + 1; }
        else { const int rem = NPIECE - 1 - f; if (rem < W) { if (rem == 1) wait_vm<4>(); else if (rem == 2) wait_vm<8>(); else if (rem == 3) wait_vm<12>(); else wait_vm<0>(); } }
        const LAS char* kslot = (const LAS char*)ring + cs * 4096; cs = cs + 1 == S ? 0 : cs + 1;
        v4f sc = {0, 0, 0, 0};
#pragma unroll
        for (int s = 0; s < 4; ++s) { const v8bf kf = *(const LAS v8bf*)(kslot + kra[s]); sc = __builtin_amdgcn_mfma_f32_16x16x32_bf16(kf, qf[s], sc, 0, 0, 0); }
        float t0 = sc[0] * C2, t1 = sc[1] * C2, t2 = sc[2] * C2, t3 = sc[3] * C2;
        const float mx = xrow16_max(fmaxf(fmaxf(t0, t1), fmaxf(t2, t3)));
        const float mn = fmaxf(m, mx), alpha = __builtin_amdgcn_exp2f(m - mn);
        const float p0 = __builtin_amdgcn_exp2f(t0 - mn), p1 = __builtin_amdgcn_exp2f(t1 - mn), p2 = __builtin_amdgcn_exp2f(t2 - mn), p3 = __builtin_amdgcn_exp2f(t3 - mn);
        l = l * alpha + ((p0 + p1) + (p2 + p3)); m = mn;
        const unsigned pa = pk(p0, p1), pb = pk(p2, p3);
        const v4s pf = __builtin_bit_cast(v4s, (unsigned __attribute__((ext_vector_type(2)))){pa, pb});
        if (f + 1 + A < NPIECE) { wait_vm<4 * (W - 1)>(); issue(f + 1 + A, is); is = is + 1 == S ? 0 : is + 1; }
        else { const int rem = NPIECE - 2 - f; if (rem < W) { if (rem == 1) wait_vm<4>(); else if (rem == 2) wait_vm<8>(); else if (rem == 3) wait_vm<12>(); else wait_vm<0>(); } }
        const unsigned vs = (unsigned)(cs * 4096); cs = cs + 1 == S ? 0 : cs + 1;
        v4s vf[8];
        { const unsigned b = ring_u + vs;
          asm volatile("ds_read_b64_tr_b16 %0, %8\n\tds_read_b64_tr_b16 %1, %9\n\tds_read_b64_tr_b16 %2, %10\n\tds_read_b64_tr_b16 %3, %11\n\t"
                       "ds_read_b64_tr_b16 %4, %12\n\tds_read_b64_tr_b16 %5, %13\n\tds_read_b64_tr_b16 %6, %14\n\tds_read_b64_tr_b16 %7, %15\n\ts_waitcnt lgkmcnt(0)"
                       : "=&v"(vf[0]), "=&v"(vf[1]), "=&v"(vf[2]), "=&v"(vf[3]), "=&v"(vf[4]), "=&v"(vf[5]), "=&v"(vf[6]), "=&v"(vf[7])
                       : "v"(b + vra), "v"(b + (vra ^ 32)), "v"(b + (vra ^ 64)), "v"(b + (vra ^ 96)), "v"(b + (vra ^ 128)), "v"(b + (vra ^ 160)), "v"(b + (vra ^ 192)), "v"(b + (vra ^ 224))
                       : "memory"); }
#pragma unroll
        for (int c = 0; c < 8; ++c) { o[c] *= alpha; o[c] = __builtin_amdgcn_mfma_f32_16x16x16bf16_1k(vf[c], pf, o[c], 0, 0, 0); }
    }
    l = xrow16_sum(l);
    asm volatile("s_waitcnt vmcnt(0) lgkmcnt(0)" ::: "memory");
    __syncthreads();
    float* mrg = (float*)lds;
    if (c16 < 4) { float* rec = mrg + (wave * 4 + c16) * 132;
#pragma unroll
        for (int c = 0; c < 8; ++c) *(v4f*)(rec + 16 * c + 4 * g) = o[c];
        if (g == 0) { rec[128] = m; rec[129] = l; } }
    __syncthreads();
    float* part = (float*)(ws + WS_PART) + (size_t)(h * NSPLIT + j) * 4 * 132;
    { const int hh = tid >> 6, d = (tid & 63) * 2; float M = -INFINITY;
#pragma unroll
      for (int w = 0; w < 4; ++w) M = fmaxf(M, mrg[(w * 4 + hh) * 132 + 128]);
      float L = 0, a0 = 0, a1 = 0;
#pragma unroll
      for (int w = 0; w < 4; ++w) { const float* rec = mrg + (w * 4 + hh) * 132; const float wt = __builtin_amdgcn_exp2f(rec[128] - M); L += wt * rec[129]; a0 += wt * rec[d]; a1 += wt * rec[d + 1]; }
      *(float2*)(part + hh * 132 + d) = make_float2(a0, a1);
      if ((tid & 63) == 0) *(float2*)(part + hh * 132 + 128) = make_float2(M, L); }
    (void)ring_u;
}

__global__ void __launch_bounds__(256) k_post1(const bfr* wo, const bfr* w2, char* ws) {
    __shared__ float ov[128];
    const int tid = threadIdx.x, b = blockIdx.x;
    const bfr* wmat;
    if (b < 32) { const int h = b >> 2, hh = b & 3;
        if (tid < 128) { const float* part = (const float*)(ws + WS_PART) + (size_t)h * NSPLIT * 4 * 132 + hh * 132; float M = -INFINITY;
            for (int j = 0; j < NSPLIT; ++j) M = fmaxf(M, part[j * 4 * 132 + 128]);
            float L = 0, a = 0; for (int j = 0; j < NSPLIT; ++j) { const float* r = part + j * 4 * 132; const float wt = __builtin_amdgcn_exp2f(r[128] - M); L += wt * r[129]; a += wt * r[tid]; }
            ov[tid] = rb(a / L); }
        wmat = wo + (size_t)b * 128 * D;
    } else { if (tid < 128) ov[tid] = b2f(((const bfr*)(ws + WS_H))[(b - 32) * 128 + tid]); wmat = w2 + (size_t)(b - 32) * 128 * D; }
    __syncthreads();
    float a0 = 0, a1 = 0;
    for (int r = 0; r < 128; ++r) { const unsigned w = *(const unsigned*)(wmat + (size_t)r * D + tid * 2); a0 += ov[r] * __builtin_bit_cast(float, w << 16); a1 += ov[r] * __builtin_bit_cast(float, w & 0xffff0000u); }
    *(float2*)((float*)(ws + WS_SP) + b * D + tid * 2) = make_float2(a0, a1);
}
__global__ void __launch_bounds__(512) k_post2(const bfr* x, char* ws, bfr* out) {
    const int n = threadIdx.x; const float* sp = (const float*)(ws + WS_SP);
    float s = 0, g = 0; for (int b = 0; b < 32; ++b) s += sp[b * D + n]; for (int b = 32; b < 46; ++b) g += sp[b * D + n];
    const float gb = rb(g), sg = rb(gb / (1.f + __expf(-gb)));
    out[n] = f2b(b2f(x[n]) + rb(rb(s) * sg));
}

extern "C" void kernel_launch(void* const* d_in, const int* in_sizes, int n_in, void* d_out, int out_size, void* d_ws, size_t ws_size, hipStream_t stream) {
    constexpr int LDS = NWAVE * S * 4096 > 84 * 1024 ? NWAVE * S * 4096 : 84 * 1024;
    hipFuncSetAttribute((const void*)k_attn, hipFuncAttributeMaxDynamicSharedMemorySize, LDS);
    k_pre<<<256, 256, 0, stream>>>((const bfr*)d_in[0], (const bfr*)d_in[3], (const bfr*)d_in[6], (char*)d_ws);
    k_attn<<<256, 256, LDS, stream>>>((const char*)d_in[1], (const char*)d_in[2], (char*)d_ws);
    k_post1<<<46, 256, 0, stream>>>((const bfr*)d_in[4], (const bfr*)d_in[7], (char*)d_ws);
    k_post2<<<1, 512, 0, stream>>>((const bfr*)d_in[0], (char*)d_ws, (bfr*)d_out);
}
```

```cpp
#include <hip/hip_runtime.h>
#include <hip/hip_bf16.h>
#include <cstdint>
#define GAS __attribute__((address_space(1)))
#define LAS __attribute__((address_space(3)))
typedef unsigned short bfr;
typedef unsigned long long u64;
typedef unsigned v4u __attribute__((ext_vector_type(4)));
typedef unsigned v2u __attribute__((ext_vector_type(2)));
typedef float v4f __attribute__((ext_vector_type(4)));
typedef short v4s __attribute__((ext_vector_type(4)));
typedef __bf16 v8bf __attribute__((ext_vector_type(8)));
typedef __bf16 v2bf __attribute__((ext_vector_type(2)));

constexpr int D = 512, SK = 65536, DFF = 1792;
constexpr int NSW = 4, UNITS = 32, NPIECE = 2 * UNITS;
constexpr int S = 9, W = 2, A = S - 1;
constexpr int T_P3 = 5, T_HP = 9;
static_assert(W <= A && A <= 8, "window");
constexpr int KV_POLICY = 2;
constexpr float C2 = 0.08838834764831845f * 1.4426950408889634f;
constexpr unsigned SENT = 0xFFFFFFFFu;
constexpr int SPIN_CAP = 1 << 15;

constexpr unsigned WS_EPOCH = 0;
constexpr unsigned WS_QG = 4096;
constexpr unsigned WS_H1 = WS_QG + 4096 * 8;
constexpr unsigned WS_ML = WS_H1 + 8 * 4 * 32 * 128 * 4;
constexpr unsigned WS_H2 = WS_ML + 8 * 4 * 32 * 8;
constexpr unsigned WS_HP = WS_H2 + 512 * 128 * 4;
constexpr unsigned WS_G = WS_HP + 28 * 8 * 64 * 4;
constexpr unsigned WS_END = WS_G + 28 * 512 * 4;
constexpr int L_RING = 0, L_CTRL = NSW * S * 4096;
constexpr int L_QCNT = L_CTRL, L_QRDY = L_CTRL + 4, L_CLR = L_CTRL + 8, L_PROG = L_CTRL + 12;
constexpr int L_OB = L_CTRL + 64;
constexpr int L_FIN = L_OB + 128;
constexpr int L_QPART = L_FIN + 16;
constexpr int L_QS = L_QPART + 256;
constexpr int L_HB = L_QS + 1024;
constexpr int LDS_BYTES = L_HB + 256;
static_assert(LDS_BYTES <= 163840 && L_HB % 16 == 0, "LDS");

__device__ __forceinline__ float b2f(bfr b) { return __builtin_bit_cast(float, (unsigned)b << 16); }
__device__ __forceinline__ float blo(unsigned u) { return __builtin_bit_cast(float, u << 16); }
__device__ __forceinline__ float bhi(unsigned u) { return __builtin_bit_cast(float, u & 0xffff0000u); }
__device__ __forceinline__ bfr f2b(float f) { return __builtin_bit_cast(bfr, (__bf16)f); }
__device__ __forceinline__ float rb(float f) { return (float)(__bf16)f; }
__device__ __forceinline__ unsigned pk(float lo, float hi) { v2bf r = {(__bf16)lo, (__bf16)hi}; return __builtin_bit_cast(unsigned, r); }
__device__ __forceinline__ unsigned fb(float f) { return __builtin_bit_cast(unsigned, f); }
__device__ __forceinline__ float bf(unsigned u) { return __builtin_bit_cast(float, u); }
__device__ __forceinline__ unsigned nosent(float f) { return min(fb(f), SENT - 1u); }
__device__ __forceinline__ v4u nosent4(float a, float b, float c, float d) { return v4u{nosent(a), nosent(b), nosent(c), nosent(d)}; }
template <int CTRL> __device__ __forceinline__ float dpp(float x) { return bf(__builtin_amdgcn_mov_dpp(fb(x), CTRL, 0xf, 0xf, true)); }
constexpr int XOR1 = 0xB1, XOR2 = 0x4E, HMIR = 0x141, ROR2 = 0x122, ROR4 = 0x124, ROR8 = 0x128;
__device__ __forceinline__ float x16sum(float x) { auto s = __builtin_amdgcn_permlane16_swap(fb(x), fb(x), false, false); return bf(s[0]) + bf(s[1]); }
__device__ __forceinline__ float x32sum(float x) { auto s = __builtin_amdgcn_permlane32_swap(fb(x), fb(x), false, false); return bf(s[0]) + bf(s[1]); }
__device__ __forceinline__ float x16max(float x) { auto s = __builtin_amdgcn_permlane16_swap(fb(x), fb(x), false, false); return fmaxf(bf(s[0]), bf(s[1])); }
__device__ __forceinline__ float x32max(float x) { auto s = __builtin_amdgcn_permlane32_swap(fb(x), fb(x), false, false); return fmaxf(bf(s[0]), bf(s[1])); }
__device__ __forceinline__ float xrow_max(float x) { return x32max(x16max(x)); }
__device__ __forceinline__ float xrow_sum(float x) { return x32sum(x16sum(x)); }
__device__ __forceinline__ float row_sum16(float x) { x += dpp<XOR1>(x); x += dpp<XOR2>(x); x += dpp<HMIR>(x); x += dpp<ROR8>(x); return x; }
__device__ __forceinline__ float wave_sum(float x) { return xrow_sum(row_sum16(x)); }
__device__ __forceinline__ float sum_hi3(float v) { v += dpp<ROR8>(v); return xrow_sum(v); }

__device__ __forceinline__ void st16(char* p, v4u v) { asm volatile("global_store_dwordx4 %0, %1, off sc1\n\ts_nop 1" :: "v"(p), "v"(v) : "memory"); }
__device__ __forceinline__ void st8(char* p, u64 v) { __hip_atomic_store((GAS u64*)p, v, __ATOMIC_RELAXED, __HIP_MEMORY_SCOPE_AGENT); }
__device__ __forceinline__ u64 ld8(const char* p) { return __hip_atomic_load((GAS u64*)p, __ATOMIC_RELAXED, __HIP_MEMORY_SCOPE_AGENT); }
__device__ __forceinline__ unsigned umax4(v4u v) { return max(max(v[0], v[1]), max(v[2], v[3])); }
__device__ __forceinline__ bool has_sent(v4u v) { return umax4(v) == SENT; }
template <int N> __device__ __forceinline__ void wait_vm() { asm volatile("s_waitcnt vmcnt(%0) lgkmcnt(0)" :: "i"(N) : "memory"); }
__device__ __forceinline__ void wait_rem(int rem) {
    switch (rem) { case 0: wait_vm<0>(); break; case 1: wait_vm<4>(); break; case 2: wait_vm<8>(); break; case 3: wait_vm<12>(); break;
                   case 4: wait_vm<16>(); break; case 5: wait_vm<20>(); break; case 6: wait_vm<24>(); break; default: wait_vm<28>(); break; } }
__device__ __forceinline__ unsigned lds_ld(int off) { return *(volatile LAS unsigned*)(size_t)off; }
__device__ __forceinline__ void lds_st(int off, unsigned v) { *(volatile LAS unsigned*)(size_t)off = v; }
__device__ __forceinline__ void lds_wait_ge(int off, unsigned v) { for (int sp = 0; lds_ld(off) < v && sp < SPIN_CAP * 8; ++sp) __builtin_amdgcn_s_sleep(4); asm volatile("" ::: "memory"); }
template <int SLEEP> __device__ __forceinline__ void lds_wait(int off) { for (int sp = 0; lds_ld(off) == 0 && sp < SPIN_CAP * 8; ++sp) __builtin_amdgcn_s_sleep(SLEEP); asm volatile("" ::: "memory"); }
__device__ __forceinline__ v4u ldnt(const bfr* p) { return __builtin_nontemporal_load((const v4u*)p); }
__device__ __forceinline__ void poll2(v4u& a, v4u& b, const char* pa, const char* pb, u64 ma, u64 mb) { u64 sv;
    asm volatile("s_mov_b64 %2, exec\n\ts_mov_b64 exec, %5\n\tglobal_load_dwordx4 %0, %3, off sc1\n\ts_mov_b64 exec, %6\n\tglobal_load_dwordx4 %1, %4, off sc1\n\t"
                 "s_mov_b64 exec, %2\n\ts_waitcnt vmcnt(0)" : "+v"(a), "+v"(b), "=&s"(sv) : "v"(pa), "v"(pb), "s"(ma), "s"(mb) : "memory"); }
__device__ __forceinline__ void poll41(v4u (&v)[4], v2u& ml, const char* p0, const char* p1, const char* p2, const char* p3, const char* pm, u64 m0, u64 m1, u64 m2, u64 m3, u64 mm) { u64 sv;
    asm volatile("s_mov_b64 %5, exec\n\ts_mov_b64 exec, %15\n\tglobal_load_dwordx2 %4, %10, off sc1\n\ts_mov_b64 exec, %11\n\tglobal_load_dwordx4 %0, %6, off sc1\n\ts_mov_b64 exec, %12\n\tglobal_load_dwordx4 %1, %7, off sc1\n\t"
                 "s_mov_b64 exec, %13\n\tglobal_load_dwordx4 %2, %8, off sc1\n\ts_mov_b64 exec, %14\n\tglobal_load_dwordx4 %3, %9, off sc1\n\t"
                 "s_mov_b64 exec, %5\n\ts_waitcnt vmcnt(0)" : "+v"(v[0]), "+v"(v[1]), "+v"(v[2]), "+v"(v[3]), "+v"(ml), "=&s"(sv)
                 : "v"(p0), "v"(p1), "v"(p2), "v"(p3), "v"(pm), "s"(m0), "s"(m1), "s"(m2), "s"(m3), "s"(mm) : "memory"); }
__device__ __forceinline__ void poll1(v4u& a, const char* pa, u64 ma) { u64 sv;
    asm volatile("s_mov_b64 %1, exec\n\ts_mov_b64 exec, %3\n\tglobal_load_dwordx4 %0, %2, off sc1\n\ts_mov_b64 exec, %1\n\ts_waitcnt vmcnt(0)" : "+v"(a), "=&s"(sv) : "v"(pa), "s"(ma) : "memory"); }

struct LN { float mb, sq; };
__device__ __forceinline__ LN ln_stats(v4u xv) {
    float f[8];
#pragma unroll
    for (int i = 0; i < 4; ++i) { f[2 * i] = blo(xv[i]); f[2 * i + 1] = bhi(xv[i]); }
    float s = ((f[0] + f[1]) + (f[2] + f[3])) + ((f[4] + f[5]) + (f[6] + f[7]));
    const float mean = wave_sum(s) * (1.f / D);
    float v = 0;
#pragma unroll
    for (int i = 0; i < 8; ++i) { const float d = f[i] - mean; v = fmaf(d, d, v); }
    const float var = wave_sum(v) * (1.f / D);
    return LN{rb(mean), rb(sqrtf(rb(rb(var) + 1e-5f)))};
}
__device__ __forceinline__ float ln_apply(float x, LN s) { return rb(rb(x - s.mb) / s.sq); }
__device__ __forceinline__ void fma8(float (&acc)[8], float s, v4u w) {
#pragma unroll
    for (int k = 0; k < 4; ++k) { acc[2 * k] = fmaf(s, blo(w[k]), acc[2 * k]); acc[2 * k + 1] = fmaf(s, bhi(w[k]), acc[2 * k + 1]); } }

__global__ void __launch_bounds__(512, 2) fused(const bfr* __restrict__ x, const char* __restrict__ K, const char* __restrict__ V, const bfr* __restrict__ wq,
                                                const bfr* __restrict__ wo, const bfr* __restrict__ w1, const bfr* __restrict__ w2, char* __restrict__ ws, bfr* __restrict__ out) {
    const int tid = threadIdx.x, wave = __builtin_amdgcn_readfirstlane(tid >> 6), lane = tid & 63;
    const int w = blockIdx.x, h = (w >> 3) & 7, j = (w & 7) * 4 + (w >> 6);
    const int g = lane >> 4, c16 = lane & 15;
    const bool is_final = (((w >> 3) ^ w) & 1) == 0;
    const int fidx = ((w >> 4) << 3) | (w & 7);
    if (tid < 52) lds_st(L_CTRL + 4 * tid, tid < 16 ? 0u : SENT);
    asm volatile("" :: "s"(x), "s"(K), "s"(V), "s"(wq), "s"(wo), "s"(w1), "s"(w2), "s"(ws), "s"(out));
    asm volatile("s_waitcnt lgkmcnt(0)\n\ts_barrier" ::: "memory");
    if (__builtin_expect(wave < NSW, 0)) {
        auto ko = [&](int i) -> unsigned { const int row = 4 * i + g; return row * 256 + ((c16 ^ row) << 4); };
        auto vo = [&](int i) -> unsigned { const int row = 4 * i + g; return row * 256 + ((c16 ^ ((row & 7) << 1)) << 4); };
        const unsigned k0 = ko(0), k1 = ko(1), k2 = ko(2), k3 = ko(3), v0 = vo(0), v1 = vo(1), v2 = vo(2), v3 = vo(3);
        const size_t base = ((size_t)h * SK + (size_t)j * 2048 + wave * 16) * 256;
        const char* kb = K + base; const char* vb = V + base;
        LAS char* ring = (LAS char*)(size_t)(L_RING + wave * (S * 4096));
        auto issue = [&](int f, int slot) {
            const bool isV = f & 1;
            LAS char* dst = ring + slot * 4096;
            const char* src = (isV ? vb : kb) + (unsigned)(f >> 1) * (NSW * 4096u);
            const unsigned o0 = isV ? v0 : k0, o1 = isV ? v1 : k1, o2 = isV ? v2 : k2, o3 = isV ? v3 : k3;
            __builtin_amdgcn_global_load_lds((const GAS unsigned*)(src + o0), (LAS unsigned*)(dst), 16, 0, KV_POLICY);
            __builtin_amdgcn_global_load_lds((const GAS unsigned*)(src + o1), (LAS unsigned*)(dst + 1024), 16, 0, KV_POLICY);
            __builtin_amdgcn_global_load_lds((const GAS unsigned*)(src + o2), (LAS unsigned*)(dst + 2048), 16, 0, KV_POLICY);
            __builtin_amdgcn_global_load_lds((const GAS unsigned*)(src + o3), (LAS unsigned*)(dst + 3072), 16, 0, KV_POLICY); };
#pragma unroll
        for (int f = 0; f < A; ++f) { if (f >= W) wait_vm<4 * (W - 1)>(); issue(f, f); }
        lds_wait<1>(L_QRDY);
        v8bf qf[4];
#pragma unroll
        for (int s = 0; s < 4; ++s) { v4u t = *(const LAS v4u*)(size_t)(L_QS + (c16 & 3) * 256 + 64 * s + 16 * g); if (c16 >= 4) t = v4u{0, 0, 0, 0}; qf[s] = __builtin_bit_cast(v8bf, t); }
        unsigned kra[4];
#pragma unroll
        for (int s = 0; s < 4; ++s) kra[s] = c16 * 256 + (((4 * s + g) ^ c16) << 4);
        const int vrow = 4 * g + (c16 >> 2);
        const unsigned vra = vrow * 256 + ((vrow & 7) << 5) + 8 * (c16 & 3);
        const unsigned ring_u = L_RING + wave * (S * 4096);
        float m = -INFINITY, l = 0.f; v4f o[8];
#pragma unroll
        for (int c = 0; c < 8; ++c) o[c] = v4f{0, 0, 0, 0};
        int cs = 0, is = A % S, nx = A;
        auto advance = [&](int f, bool isK) {
            if (nx >= NPIECE) { wait_rem(NPIECE - 1 - f); return; }
            if (nx - f > W && !isK) return;
            wait_vm<4 * (W - 1)>();
            issue(nx, is); ++nx; is = is + 1 == S ? 0 : is + 1; };
        for (int t = 0; t < UNITS; ++t) {
            const int f = 2 * t;
            if (wave == 0 && lane == 0) lds_st(L_PROG, t);
            advance(f, true);
            const LAS char* kslot = ring + cs * 4096; cs = cs + 1 == S ? 0 : cs + 1;
            v4f sc = {0, 0, 0, 0};
#pragma unroll
            for (int s = 0; s < 4; ++s) { const v8bf kf = *(const LAS v8bf*)(kslot + kra[s]); sc = __builtin_amdgcn_mfma_f32_16x16x32_bf16(kf, qf[s], sc, 0, 0, 0); }
            const float t0 = sc[0] * C2, t1 = sc[1] * C2, t2 = sc[2] * C2, t3 = sc[3] * C2;
            const float mx = xrow_max(fmaxf(fmaxf(t0, t1), fmaxf(t2, t3)));
            const float mn = fmaxf(m, mx), alpha = __builtin_amdgcn_exp2f(m - mn);
            const float p0 = __builtin_amdgcn_exp2f(t0 - mn), p1 = __builtin_amdgcn_exp2f(t1 - mn), p2 = __builtin_amdgcn_exp2f(t2 - mn), p3 = __builtin_amdgcn_exp2f(t3 - mn);
            l = l * alpha + ((p0 + p1) + (p2 + p3)); m = mn;
            const v4s pf = __builtin_bit_cast(v4s, v2u{pk(p0, p1), pk(p2, p3)});
            advance(f + 1, false);
            const unsigned vsb = ring_u + cs * 4096; cs = cs + 1 == S ? 0 : cs + 1;
            v4s vf[8];
            asm volatile("ds_read_b64_tr_b16 %0, %8\n\tds_read_b64_tr_b16 %1, %9\n\tds_read_b64_tr_b16 %2, %10\n\tds_read_b64_tr_b16 %3, %11\n\t"
                         "ds_read_b64_tr_b16 %4, %12\n\tds_read_b64_tr_b16 %5, %13\n\tds_read_b64_tr_b16 %6, %14\n\tds_read_b64_tr_b16 %7, %15\n\ts_waitcnt lgkmcnt(0)"
                         : "=&v"(vf[0]), "=&v"(vf[1]), "=&v"(vf[2]), "=&v"(vf[3]), "=&v"(vf[4]), "=&v"(vf[5]), "=&v"(vf[6]), "=&v"(vf[7])
                         : "v"(vsb + vra), "v"(vsb + (vra ^ 32)), "v"(vsb + (vra ^ 64)), "v"(vsb + (vra ^ 96)), "v"(vsb + (vra ^ 128)), "v"(vsb + (vra ^ 160)), "v"(vsb + (vra ^ 192)), "v"(vsb + (vra ^ 224))
                         : "memory");
#pragma unroll
            for (int c = 0; c < 8; ++c) { o[c] *= alpha; o[c] = __builtin_amdgcn_mfma_f32_16x16x16bf16_1k(vf[c], pf, o[c], 0, 0, 0); }
        }
        l = xrow_sum(l);
        if (c16 < 4) { LAS float* rec = (LAS float*)ring + c16 * 132;
#pragma unroll
            for (int c = 0; c < 8; ++c) *(LAS v4f*)(rec + 16 * c + 4 * g) = o[c];
            if (g == 0) { rec[128] = m; rec[129] = l; } }
        const char* const gp = ws + WS_G + ((lane < 28 ? lane : 0) * 512 + 4 * fidx) * 4;
        asm volatile("s_waitcnt vmcnt(0) lgkmcnt(0)\n\ts_barrier" :: "v"(gp) : "memory");
        if (wave == 0 && is_final) {
            v4u g0 = lane < 28 ? v4u{SENT, SENT, SENT, SENT} : v4u{0, 0, 0, 0};
            for (int sp = 0;;) { const u64 m0 = __ballot(has_sent(g0));
                if (m0 == 0 || ++sp > SPIN_CAP) break;
                poll1(g0, gp, m0); }
            v4u sg;
#pragma unroll
            for (int k = 0; k < 4; ++k) { const float gb = rb(wave_sum(bf(g0[k]))); sg[k] = nosent(rb(gb / (1.f + __expf(-gb)))); }
            if (lane == 0) *(LAS v4u*)(size_t)L_FIN = sg;
        }
    } else {
        const int a = wave - NSW;
        __builtin_amdgcn_s_setprio(1);
        if (a == 1) {
            constexpr int SHARE = (WS_END - WS_H1) / 256;
            static_assert(SHARE * 256 == WS_END - WS_H1 && SHARE % 16 == 0 && SHARE <= 4096, "clear share");
            char* cp = ws + WS_H1 + w * SHARE + lane * 16;
#pragma unroll
            for (int i = 0; i < 4; ++i) if (lane * 16 + i * 1024 < SHARE) st16(cp + i * 1024, v4u{SENT, SENT, SENT, SENT});
        }
        const v4u xv = *(const v4u*)(x + lane * 8);
        const int r0 = 128 * a + (lane >> 1);
        bfr xr[4]; v4u wv[4];
#pragma unroll
        for (int i = 0; i < 4; ++i) xr[i] = x[r0 + 32 * i];
#pragma unroll
        for (int i = 0; i < 4; ++i) wv[i] = *(const v4u*)(wq + (size_t)(r0 + 32 * i) * 4096 + h * 512 + j * 16 + (lane & 1) * 8);
        unsigned zoff = WS_EPOCH;
        asm volatile("" : "+v"(zoff));
        const u64 epoch = ld8(ws + zoff);
        const u64 tag = epoch + 1;
        const int cg = w >> 3, ks = w & 7;
        const LN st = ln_stats(xv);
        {
            float acc[8] = {};
#pragma unroll
            for (int i = 0; i < 4; ++i) fma8(acc, ln_apply(b2f(xr[i]), st), wv[i]);
#pragma unroll
            for (int k = 0; k < 8; ++k) { float v = acc[k]; v += dpp<ROR2>(v); v += dpp<ROR4>(v); v += dpp<ROR8>(v); acc[k] = xrow_sum(v); }
            if (lane < 2) {
                *(LAS v4f*)(size_t)(L_QPART + a * 64 + lane * 32) = v4f{acc[0], acc[1], acc[2], acc[3]};
                *(LAS v4f*)(size_t)(L_QPART + a * 64 + lane * 32 + 16) = v4f{acc[4], acc[5], acc[6], acc[7]}; }
            asm volatile("s_waitcnt vmcnt(0) lgkmcnt(0)" ::: "memory");
            unsigned old = 0;
            if (lane == 0) old = __hip_atomic_fetch_add((LAS unsigned*)(size_t)L_QCNT, 1u, __ATOMIC_RELAXED, __HIP_MEMORY_SCOPE_WORKGROUP);
            old = __builtin_amdgcn_readfirstlane(old);
            if (old == 3) {
                asm volatile("" ::: "memory");
                if (lane < 16) { float q = 0;
#pragma unroll
                    for (int aa = 0; aa < 4; ++aa) q += *(volatile LAS float*)(size_t)(L_QPART + aa * 64 + lane * 4);
                    st8(ws + WS_QG + (h * 512 + j * 16 + lane) * 8, (tag << 16) | f2b(q)); }
            }
        }
        v4u gpart = {0, 0, 0, 0};
        if (a == 0) {
            int sp = 0;
            const unsigned tlo = (unsigned)(tag << 16) >> 16, thi = (unsigned)(tag >> 16);
            const char* qp = ws + WS_QG + (h * 512 + lane) * 8;
#define QLOADS(q, mk) asm volatile("s_mov_b64 %8, exec\n\ts_mov_b64 exec, %10\n\tglobal_load_dwordx2 %0, %9, off sc1\n\ts_mov_b64 exec, %11\n\tglobal_load_dwordx2 %1, %9, off offset:512 sc1\n\t" \
                             "s_mov_b64 exec, %12\n\tglobal_load_dwordx2 %2, %9, off offset:1024 sc1\n\ts_mov_b64 exec, %13\n\tglobal_load_dwordx2 %3, %9, off offset:1536 sc1\n\t" \
                             "s_mov_b64 exec, %14\n\tglobal_load_dwordx2 %4, %9, off offset:2048 sc1\n\ts_mov_b64 exec, %15\n\tglobal_load_dwordx2 %5, %9, off offset:2560 sc1\n\t" \
                             "s_mov_b64 exec, %16\n\tglobal_load_dwordx2 %6, %9, off offset:3072 sc1\n\ts_mov_b64 exec, %17\n\tglobal_load_dwordx2 %7, %9, off offset:3584 sc1\n\t" \
                             "s_mov_b64 exec, %8\n\ts_waitcnt vmcnt(0)" \
                             : "+v"(q[0]), "+v"(q[1]), "+v"(q[2]), "+v"(q[3]), "+v"(q[4]), "+v"(q[5]), "+v"(q[6]), "+v"(q[7]), "=&s"(sv) \
                             : "v"(qp), "s"(mk[0]), "s"(mk[1]), "s"(mk[2]), "s"(mk[3]), "s"(mk[4]), "s"(mk[5]), "s"(mk[6]), "s"(mk[7]) : "memory")
#define QMASKS(q, mk, any) do { any = 0; _Pragma("unroll") for (int i = 0; i < 8; ++i) { mk[i] = __ballot((q[i][0] >> 16) != tlo || q[i][1] != thi); any |= mk[i]; } } while (0)
            v2u qa[8]; u64 ma[8], any, sv;
#pragma unroll
            for (int i = 0; i < 8; ++i) { qa[i] = v2u{~tlo << 16, ~thi}; ma[i] = ~0ull; }
            __builtin_amdgcn_s_sleep(64);
            for (;;) { QLOADS(qa, ma); QMASKS(qa, ma, any); if (any == 0 || ++sp > SPIN_CAP) break; }
#pragma unroll
            for (int i = 0; i < 8; ++i) *(volatile LAS bfr*)(size_t)(L_QS + (lane + 64 * i) * 2) = (bfr)qa[i][0];
            asm volatile("s_waitcnt lgkmcnt(0)" ::: "memory");
            if (lane == 0) lds_st(L_QRDY, 1);
            lds_wait_ge(L_PROG, T_P3);
            for (sp = 0;;) { bool ok = true;
#pragma unroll
                for (int i = 0; i < 4; ++i) ok &= (ld8(ws + WS_QG + (lane + 64 * i) * 128) >> 16) == (tag & 0xFFFFFFFFFFFFull);
                if (__all(ok) || ++sp > SPIN_CAP) break;
                __builtin_amdgcn_s_sleep(16); }
            if (lane == 0) lds_st(L_CLR, 1);
        } else if (a == 2) {
            if (w < 224) {
                v4u bw[8]; bfr xb[8];
#pragma unroll
                for (int i = 0; i < 8; ++i) { const int r = 64 * ks + 8 * i + (lane >> 3); xb[i] = x[r]; bw[i] = ldnt(w1 + (size_t)r * DFF + 64 * cg + 8 * (lane & 7)); }
                float acc[8] = {};
#pragma unroll
                for (int i = 0; i < 8; ++i) fma8(acc, ln_apply(b2f(xb[i]), st), bw[i]);
#pragma unroll
                for (int k = 0; k < 8; ++k) acc[k] = sum_hi3(acc[k]);
                lds_wait<8>(L_CLR);
                if (lane < 16) st16(ws + WS_HP + (w * 64 + (lane & 7) * 8 + (lane >> 3) * 4) * 4, lane < 8 ? nosent4(acc[0], acc[1], acc[2], acc[3]) : nosent4(acc[4], acc[5], acc[6], acc[7]));
            }
        } else if (a == 3) {
            if (w < 224) {
                v4u bw[8];
#pragma unroll
                for (int i = 0; i < 8; ++i) bw[i] = ldnt(w2 + (size_t)(64 * cg + 8 * i + (lane >> 3)) * D + 64 * ks + 8 * (lane & 7));
                lds_wait<8>(L_CLR);
                lds_wait_ge(L_PROG, T_HP);
                const char* hp0 = ws + WS_HP + ((cg * 8 + (lane >> 3)) * 64 + (lane & 7) * 8) * 4;
                v4u h0 = {SENT, SENT, SENT, SENT}, h1 = h0;
                for (int sp = 0;;) { const u64 m0 = __ballot(has_sent(h0)), m1 = __ballot(has_sent(h1));
                    if ((m0 | m1) == 0 || ++sp > SPIN_CAP) break;
                    poll2(h0, h1, hp0, hp0 + 16, m0, m1);
                    __builtin_amdgcn_s_sleep(8); }
                float hv[8];
#pragma unroll
                for (int k = 0; k < 4; ++k) { hv[k] = fmaxf(rb(sum_hi3(bf(h0[k]))), 0.f); hv[4 + k] = fmaxf(rb(sum_hi3(bf(h1[k]))), 0.f); }
                if (lane < 8) { *(LAS v4f*)(size_t)(L_HB + lane * 32) = v4f{hv[0], hv[1], hv[2], hv[3]}; *(LAS v4f*)(size_t)(L_HB + lane * 32 + 16) = v4f{hv[4], hv[5], hv[6], hv[7]}; }
                asm volatile("s_waitcnt lgkmcnt(0)" ::: "memory");
                float acc[8] = {};
#pragma unroll
                for (int i = 0; i < 8; ++i) fma8(acc, *(volatile LAS float*)(size_t)(L_HB + (8 * i + (lane >> 3)) * 4), bw[i]);
#pragma unroll
                for (int k = 0; k < 8; ++k) acc[k] = sum_hi3(acc[k]);
                gpart = lane < 8 ? nosent4(acc[0], acc[1], acc[2], acc[3]) : nosent4(acc[4], acc[5], acc[6], acc[7]);
            }
        }
        const int hh2 = j >> 3, dq = (j >> 1) & 3, nc2 = j & 1, hq = 4 * h + hh2, pid = hq * 4 + dq;
        const int d4 = lane & 7, jq = lane >> 3;
        float wof[4][8];
        {   v4u wov[4];
#pragma unroll
            for (int i = 0; i < 4; ++i) wov[i] = ldnt(wo + (size_t)(128 * hq + 32 * dq + 4 * d4 + i) * D + 256 * nc2 + 64 * a + 8 * jq);
#pragma unroll
            for (int i = 0; i < 4; ++i)
#pragma unroll
                for (int k = 0; k < 4; ++k) { wof[i][2 * k] = blo(wov[i][k]); wof[i][2 * k + 1] = bhi(wov[i][k]); asm volatile("" : "+v"(wof[i][2 * k]), "+v"(wof[i][2 * k + 1])); } }
        const int mh = 2 * (a & 1) + (lane >> 5);
        char* const pub1 = a < 2 ? ws + WS_H1 + (((h * 4 + mh) * 32 + j) * 128 + 4 * (lane & 31)) * 4
                         : a == 2 ? ws + WS_ML + ((h * 4 + (lane & 3)) * 32 + j) * 8 : ws + WS_G + (cg * 512 + ks * 64 + (lane & 7) * 8 + ((lane >> 3) & 1) * 4) * 4;
        const char* const hp = ws + WS_H1 + (((h * 4 + hh2) * 32 + jq) * 128 + 32 * dq + 4 * d4) * 4;
        const char* const mp = ws + WS_ML + ((h * 4 + hh2) * 32 + jq + 8 * (d4 & 3)) * 8;
        const int c0 = 256 * nc2 + 64 * a + 8 * jq + 4 * (d4 & 1);
        char* const pub2 = ws + WS_H2 + ((c0 >> 2) * 128 + pid) * 16;
        const char* const fp = ws + WS_H2 + fidx * 2048 + lane * 16;
        v2u xf = {0, 0};
        if (a == 1 && is_final) { xf = *(const v2u*)(x + 4 * fidx);
            const unsigned touch = *(const volatile unsigned*)(out + 4 * fidx);
            asm volatile("" :: "v"(touch));
        }
        asm volatile("s_waitcnt vmcnt(0)" :: "v"(pub1), "v"(hp), "v"(mp), "v"(pub2), "v"(fp), "v"(xf) : "memory");
        asm volatile("s_waitcnt vmcnt(0) lgkmcnt(0)\n\ts_barrier" ::: "memory");
        if (a < 3) {
            const int hd = a < 2 ? mh : (lane & 3);
            float mw[4], lw[4]; v4f ow[4];
#pragma unroll
            for (int ww = 0; ww < 4; ++ww) { const LAS float* rec = (const LAS float*)(size_t)(L_RING + ww * (S * 4096)) + hd * 132; mw[ww] = rec[128]; lw[ww] = rec[129]; ow[ww] = *(const LAS v4f*)(rec + 4 * (lane & 31)); }
            const float M = fmaxf(fmaxf(mw[0], mw[1]), fmaxf(mw[2], mw[3]));
            float L = 0; v4f acc = {0, 0, 0, 0};
#pragma unroll
            for (int ww = 0; ww < 4; ++ww) { const float wt = __builtin_amdgcn_exp2f(mw[ww] - M); L = fmaf(wt, lw[ww], L); acc += wt * ow[ww]; }
            if (a < 2) st16(pub1, nosent4(acc[0], acc[1], acc[2], acc[3]));
            else if (lane < 4) st8(pub1, ((u64)nosent(L) << 32) | nosent(M));
        } else if (w < 224 && lane < 16) st16(pub1, gpart);
        if (a == 0) {
            v4u pv[4] = {}; v2u ml = {}; u64 m0 = ~0ull, m1 = ~0ull, m2 = ~0ull, m3 = ~0ull, mm = ~0ull;
            int sp = 0;
            do { poll41(pv, ml, hp, hp + 8 * 512, hp + 16 * 512, hp + 24 * 512, mp, m0, m1, m2, m3, mm);
                 m0 = __ballot(has_sent(pv[0])); m1 = __ballot(has_sent(pv[1])); m2 = __ballot(has_sent(pv[2])); m3 = __ballot(has_sent(pv[3])); mm = __ballot(max(ml[0], ml[1]) == SENT);
            } while ((m0 | m1 | m2 | m3 | mm) != 0 && ++sp < SPIN_CAP);
            float M = bf(ml[0]);
            M = fmaxf(M, dpp<XOR1>(M)); M = fmaxf(M, dpp<XOR2>(M)); M = fmaxf(M, dpp<ROR8>(M)); M = xrow_max(M);
            const float wj = __builtin_amdgcn_exp2f(bf(ml[0]) - M);
            float L = wj * bf(ml[1]); L += dpp<XOR1>(L); L += dpp<XOR2>(L);
            v4f o = dpp<0x00>(wj) * __builtin_bit_cast(v4f, pv[0]);
            o += dpp<0x55>(wj) * __builtin_bit_cast(v4f, pv[1]); o += dpp<0xAA>(wj) * __builtin_bit_cast(v4f, pv[2]); o += dpp<0xFF>(wj) * __builtin_bit_cast(v4f, pv[3]);
            const float inv = __builtin_amdgcn_rcpf(sum_hi3(L));
            v4f ob;
#pragma unroll
            for (int k = 0; k < 4; ++k) ob[k] = bf(nosent(rb(sum_hi3(o[k]) * inv)));
            if (lane < 8) *(LAS v4f*)(size_t)(L_OB + lane * 16) = ob;
        }
        {
            v4u obu; int sp = 0;
            do { obu = *(const volatile LAS v4u*)(size_t)(L_OB + d4 * 16); } while (__any(has_sent(obu)) && ++sp < SPIN_CAP * 8);
            float acc[8] = {};
#pragma unroll
            for (int i = 0; i < 4; ++i)
#pragma unroll
                for (int k = 0; k < 8; ++k) acc[k] = fmaf(bf(obu[i]), wof[i][k], acc[k]);
#pragma unroll
            for (int k = 0; k < 8; ++k) { float v = acc[k]; v += dpp<XOR1>(v); v += dpp<XOR2>(v); v += dpp<HMIR>(v); acc[k] = v; }
            const bool hi = d4 & 1;
            if (d4 < 2) st16(pub2, nosent4(hi ? acc[4] : acc[0], hi ? acc[5] : acc[1], hi ? acc[6] : acc[2], hi ? acc[7] : acc[3]));
        }
        if (a == 1 && is_final) {
            v4u s0 = {}, s1 = {}; u64 m0 = ~0ull, m1 = ~0ull;
            const v4f xx = {blo(xf[0]), bhi(xf[0]), blo(xf[1]), bhi(xf[1])};
            v4u sgu;
            { int sp = 0; do { sgu = *(const volatile LAS v4u*)(size_t)L_FIN; } while (has_sent(sgu) && ++sp < SPIN_CAP * 8); }
            const v4f sg = __builtin_bit_cast(v4f, sgu);
            { int sp = 0; do { poll2(s0, s1, fp, fp + 1024, m0, m1); m0 = __ballot(has_sent(s0)); m1 = __ballot(has_sent(s1)); } while ((m0 | m1) != 0 && ++sp < SPIN_CAP); }
            float r[4];
#pragma unroll
            for (int k = 0; k < 4; ++k) r[k] = xx[k] + rb(rb(wave_sum(bf(s0[k]) + bf(s1[k]))) * sg[k]);
            if (lane == 0) { *(v2u*)(out + 4 * fidx) = v2u{pk(r[0], r[1]), pk(r[2], r[3])};
                if (fidx == 0) st8(ws + WS_EPOCH, tag); }
        }
    }
}

extern "C" void kernel_launch(void* const* d_in, const int* in_sizes, int n_in, void* d_out, int out_size, void* d_ws, size_t ws_size, hipStream_t stream) {
    (void)hipFuncSetAttribute((const void*)fused, hipFuncAttributeMaxDynamicSharedMemorySize, LDS_BYTES);
    fused<<<256, 512, LDS_BYTES, stream>>>((const bfr*)d_in[0], (const char*)d_in[1], (const char*)d_in[2], (const bfr*)d_in[3], (const bfr*)d_in[4],
                                           (const bfr*)d_in[6], (const bfr*)d_in[7], (char*)d_ws, (bfr*)d_out);
}
```
